```python
import torch

def _softmax(x, axis=-1):
    return torch.softmax(x, dim=axis)
def _gelu(x):
    return torch.nn.functional.gelu(x, approximate="tanh")
def _silu(x):
    return torch.nn.functional.silu(x)
def _sigmoid(x):
    return torch.sigmoid(x)
def _layernorm(x, axis=-1, eps=1e-5):
    m = x.mean(dim=axis, keepdim=True)
    v = x.var(dim=axis, keepdim=True, unbiased=False)
    return (x - m) / torch.sqrt(v + eps)
def _scatter_add(tgt, idx, src, axis=0):
    return tgt.clone().index_add_(axis, idx, src)
def _segment_sum(src, seg, n):
    out = torch.zeros((n,) + src.shape[1:], dtype=src.dtype)
    return out.index_add_(0, seg, src)
def _pad(x, pw):
    flat = tuple(v for pair in reversed(pw) for v in pair)
    return torch.nn.functional.pad(x, flat)

kernel_name = 'standalone__sliding_local_strided__prompt'

def setup_inputs():

    _w = (lambda a: (lambda t: t.to(torch.bfloat16) if t.is_floating_point() else t)(torch.as_tensor(a)))
    B, H, S, D = 1, 32, 2048, 192
    return {
        "q": _w((lambda *s: torch.randn(*s, dtype=torch.bfloat16) * 0.8)(B, H, S, D)),
        "k": _w((lambda *s: torch.randn(*s, dtype=torch.bfloat16) * 0.8)(B, H, S, D)),
        "v": _w((lambda *s: torch.randn(*s, dtype=torch.bfloat16) * 0.05)(B, H, S, D)),
    }

def reference(q, k, v):
    B, H, S, D = q.shape
    W, dil = 8, 4
    scale = D ** -0.5
    pos = torch.arange(S)
    kidx = pos[:, None] - torch.arange(W)[None, :] * dil
    valid = kidx >= 0
    kidx = torch.where(valid, kidx, torch.zeros_like(kidx))
    idx = torch.reshape(kidx, (1, 1, S * W, 1))
    idx = (lambda x, n, axis: torch.repeat_interleave(x, n, dim=axis))(idx, B, 0)
    idx = (lambda x, n, axis: torch.repeat_interleave(x, n, dim=axis))(idx, H, 1)
    idx = (lambda x, n, axis: torch.repeat_interleave(x, n, dim=axis))(idx, D, 3)
    kg = torch.reshape((lambda x, i, axis: torch.take_along_dim(x, i, dim=axis))(k, idx, 2), (B, H, S, W, D))
    vg = torch.reshape((lambda x, i, axis: torch.take_along_dim(x, i, dim=axis))(v, idx, 2), (B, H, S, W, D))
    scores = torch.einsum("bhsd,bhswd->bhsw", q, kg) * scale
    mask_f = (lambda x, dt: x.to(dt))(valid, q.dtype)[None, None, :, :]
    scores = scores + (mask_f - 1.0) * 1e9
    p = _softmax(scores, axis=-1)
    return torch.einsum("bhsw,bhswd->bhsd", p, vg)
```

```cpp
#include <hip/hip_runtime.h>
#include <hip/hip_bf16.h>
#include <stdint.h>

typedef __attribute__((ext_vector_type(2))) __bf16 bf16x2;
typedef __attribute__((ext_vector_type(4))) unsigned u32x4;
typedef __attribute__((ext_vector_type(2))) unsigned u32x2;

#define AUX_Q 2
#define AUX_KV 0
#define AUX_O 2

constexpr int H = 32, S = 2048, D = 192;
constexpr int ROWB = D * 2;
constexpr int SROWB = 4 * ROWB;
constexpr int L = 8;
constexpr unsigned TOTAL_BYTES = (unsigned)H * S * ROWB;

__device__ __forceinline__ float dot2(unsigned a, unsigned b, float acc) {
  return __builtin_amdgcn_fdot2_f32_bf16(__builtin_bit_cast(bf16x2, a), __builtin_bit_cast(bf16x2, b), acc, false);
}
template <int CTRL> __device__ __forceinline__ float dpp(float x) {
  return __builtin_bit_cast(float, __builtin_amdgcn_mov_dpp(__builtin_bit_cast(int, x), CTRL, 0xf, 0xf, true));
}
constexpr int XOR1 = 0xB1, XOR2 = 0x4E, XOR7 = 0x141, XOR8 = 0x128;

__device__ __forceinline__ unsigned pack_bf16(float lo, float hi) {
  bf16x2 r;
  r[0] = (__bf16)lo;
  r[1] = (__bf16)hi;
  return __builtin_bit_cast(unsigned, r);
}

struct Row { u32x4 a; u32x2 b; };

template <int AUX>
__device__ __forceinline__ Row load_row(__amdgpu_buffer_rsrc_t rs, int voffA, int voffB, int soff) {
  Row r;
  r.a = __builtin_amdgcn_raw_buffer_load_b128(rs, voffA, soff, AUX);
  r.b = __builtin_amdgcn_raw_buffer_load_b64(rs, voffB, soff, AUX);
  return r;
}

__global__ void __launch_bounds__(256, 2)
swa_kernel(const void* __restrict__ q, const void* __restrict__ k, const void* __restrict__ v, void* __restrict__ o) {
  const int lane = threadIdx.x & 63;
  const int wave = __builtin_amdgcn_readfirstlane(threadIdx.x >> 6);
  const int nb = gridDim.x;
  const int b = blockIdx.x;
  const int bb = (b & 7) * (nb >> 3) + (b >> 3);
  const int gw = bb * 4 + wave;
  const int h = gw >> 6, u = gw & 63;
  const int j0 = u * L;
  const bool bwd = (u & 1);
  const int g = lane >> 4, c = lane & 15;
  const int voffA = g * ROWB + 16 * c;
  const int voffB = g * ROWB + 256 + 8 * c;
  const int hb = h * (S * ROWB);

  const __amdgpu_buffer_rsrc_t rq = __builtin_amdgcn_make_buffer_rsrc((void*)q, 0, TOTAL_BYTES, 0x00020000);
  const __amdgpu_buffer_rsrc_t rk = __builtin_amdgcn_make_buffer_rsrc((void*)k, 0, TOTAL_BYTES, 0x00020000);
  const __amdgpu_buffer_rsrc_t rv = __builtin_amdgcn_make_buffer_rsrc((void*)v, 0, TOTAL_BYTES, 0x00020000);
  const __amdgpu_buffer_rsrc_t ro = __builtin_amdgcn_make_buffer_rsrc(o, 0, TOTAL_BYTES, 0x00020000);

  const int step = bwd ? -1 : 1;
  const int jk0 = bwd ? j0 + L - 1 : j0 - 7;
  const int jq0 = bwd ? j0 + L - 1 : j0;
  constexpr float C2 = 0.07216878364870322f * 1.4426950408889634f;
  const float mb = (u == 0) ? -1e9f * 1.4426950408889634f : 0.f;

  Row K[L + 7], V[L + 7], Q[L];
#define KOFF(i) (hb + max(jk0 + step * (i), 0) * SROWB)
#define QOFF(t) (hb + (jq0 + step * (t)) * SROWB)
#pragma unroll
  for (int i = 0; i < 8; ++i) K[i] = load_row<AUX_KV>(rk, voffA, voffB, KOFF(i));
  Q[0] = load_row<AUX_Q>(rq, voffA, voffB, QOFF(0));
#pragma unroll
  for (int i = 0; i < 8; ++i) V[i] = load_row<AUX_KV>(rv, voffA, voffB, KOFF(i));
  constexpr int PF = 3;
#pragma unroll
  for (int t = 1; t <= PF; ++t) {
    K[t + 7] = load_row<AUX_KV>(rk, voffA, voffB, KOFF(t + 7));
    Q[t] = load_row<AUX_Q>(rq, voffA, voffB, QOFF(t));
    V[t + 7] = load_row<AUX_KV>(rv, voffA, voffB, KOFF(t + 7));
  }
  __builtin_amdgcn_sched_barrier(0);

#pragma unroll
  for (int t = 0; t < L; ++t) {
    float s[8];
#pragma unroll
    for (int w = 0; w < 8; ++w) {
      const Row& kr = K[t + w];
      float d = 0.f;
      d = dot2(Q[t].a[0], kr.a[0], d);
      d = dot2(Q[t].a[1], kr.a[1], d);
      d = dot2(Q[t].a[2], kr.a[2], d);
      d = dot2(Q[t].a[3], kr.a[3], d);
      d = dot2(Q[t].b[0], kr.b[0], d);
      d = dot2(Q[t].b[1], kr.b[1], d);
      d += dpp<XOR8>(d);
      d += dpp<XOR7>(d);
      d += dpp<XOR2>(d);
      d += dpp<XOR1>(d);
      s[w] = (t + w < 7) ? fmaf(d, C2, mb) : d * C2;
    }
    float m = fmaxf(fmaxf(fmaxf(s[0], s[1]), fmaxf(s[2], s[3])), fmaxf(fmaxf(s[4], s[5]), fmaxf(s[6], s[7])));
    float sum = 0.f;
#pragma unroll
    for (int w = 0; w < 8; ++w) { s[w] = __builtin_amdgcn_exp2f(s[w] - m); sum += s[w]; }
    const float inv = __builtin_amdgcn_rcpf(sum);
    float acc[12];
#pragma unroll
    for (int e = 0; e < 12; ++e) acc[e] = 0.f;
#pragma unroll
    for (int w = 0; w < 8; ++w) {
      const Row& vr = V[t + w];
      const float p = s[w] * inv;
#pragma unroll
      for (int e = 0; e < 6; ++e) {
        const unsigned x = e < 4 ? vr.a[e] : vr.b[e - 4];
        acc[2 * e] = fmaf(p, __builtin_bit_cast(float, x << 16), acc[2 * e]);
        acc[2 * e + 1] = fmaf(p, __builtin_bit_cast(float, x & 0xffff0000u), acc[2 * e + 1]);
      }
    }
    u32x4 oa = {pack_bf16(acc[0], acc[1]), pack_bf16(acc[2], acc[3]), pack_bf16(acc[4], acc[5]), pack_bf16(acc[6], acc[7])};
    u32x2 ob = {pack_bf16(acc[8], acc[9]), pack_bf16(acc[10], acc[11])};
    __builtin_amdgcn_raw_buffer_store_b128(oa, ro, voffA, QOFF(t), AUX_O);
    __builtin_amdgcn_raw_buffer_store_b64(ob, ro, voffB, QOFF(t), AUX_O);
    if (t + PF + 1 < L) {
      const int tn = t + PF + 1;
      K[tn + 7] = load_row<AUX_KV>(rk, voffA, voffB, KOFF(tn + 7));
      Q[tn] = load_row<AUX_Q>(rq, voffA, voffB, QOFF(tn));
      V[tn + 7] = load_row<AUX_KV>(rv, voffA, voffB, KOFF(tn + 7));
    }
    __builtin_amdgcn_sched_barrier(0);
  }
}

extern "C" void kernel_launch(void* const* d_in, const int* in_sizes, int n_in,
                              void* d_out, int out_size, void* d_ws, size_t ws_size,
                              hipStream_t stream) {
  swa_kernel<<<512, 256, 0, stream>>>(d_in[0], d_in[1], d_in[2], d_out);
}
```

```cpp
#include <hip/hip_runtime.h>
#include <hip/hip_bf16.h>
#include <stdint.h>

typedef __attribute__((ext_vector_type(2))) __bf16 bf16x2;
typedef __attribute__((ext_vector_type(8))) __bf16 bf16x8;
typedef __attribute__((ext_vector_type(4))) unsigned u32x4;
typedef __attribute__((ext_vector_type(2))) unsigned u32x2;
typedef __attribute__((ext_vector_type(4))) float f32x4;

constexpr int H = 32, S = 2048, D = 192;
constexpr int ROWB = D * 2;
constexpr int JB = 4 * ROWB;
constexpr unsigned TOTAL_BYTES = (unsigned)H * S * ROWB;
constexpr int LT = 1152;
constexpr int US = 2 * LT + 1024;
constexpr int RING = 6;
constexpr int OOB = 0x40000000;
constexpr unsigned SRD_FLAGS = 0x00020000u;

#define SB() __builtin_amdgcn_sched_barrier(0)
#define CLOCK() ((unsigned)__builtin_amdgcn_s_memrealtime())
#define WAITV(n) asm volatile("s_waitcnt vmcnt(" #n ")" ::: "memory")
#define LGKM0() asm volatile("s_waitcnt lgkmcnt(0)" ::: "memory")
#define WAIT_UNTIL(t) do { const unsigned t_ = (t); while ((int)(CLOCK() - t_) < 0) {} SB(); } while (0)
#define GLDS_(voff, srd, soff, ldsa, MOD)                                                   \
  asm volatile("s_mov_b32 m0, %0\n\ts_nop 0\n\t"                                            \
               "buffer_load_dwordx4 %1, %2, %3 offen" MOD " lds\n\t"                        \
               "s_add_u32 m0, m0, 0x400\n\ts_nop 0\n\t"                                     \
               "buffer_load_dwordx4 %1, %2, %3 offen offset:128" MOD " lds\n\t"             \
               "s_add_u32 m0, m0, 0x400\n\ts_nop 0\n\t"                                     \
               "buffer_load_dwordx4 %1, %2, %3 offen offset:256" MOD " lds"                 \
               :: "s"(ldsa), "v"(voff), "s"(srd), "s"(soff) : "scc", "memory")
#define GLDS(voff, srd, soff, ldsa) GLDS_(voff, srd, soff, ldsa, "")
#define GLDS_NT(voff, srd, soff, ldsa) GLDS_(voff, srd, soff, ldsa, " nt")

__device__ __forceinline__ unsigned pack_bf16(float lo, float hi) {
  bf16x2 r;
  r[0] = (__bf16)lo;
  r[1] = (__bf16)hi;
  return __builtin_bit_cast(unsigned, r);
}
__device__ __forceinline__ float xg_max(float x) {
  auto s = __builtin_amdgcn_permlane16_swap(__float_as_uint(x), __float_as_uint(x), false, false);
  x = fmaxf(__uint_as_float(s[0]), __uint_as_float(s[1]));
  auto t = __builtin_amdgcn_permlane32_swap(__float_as_uint(x), __float_as_uint(x), false, false);
  return fmaxf(__uint_as_float(t[0]), __uint_as_float(t[1]));
}
__device__ __forceinline__ float xg_sum(float x) {
  auto s = __builtin_amdgcn_permlane16_swap(__float_as_uint(x), __float_as_uint(x), false, false);
  x = __uint_as_float(s[0]) + __uint_as_float(s[1]);
  auto t = __builtin_amdgcn_permlane32_swap(__float_as_uint(x), __float_as_uint(x), false, false);
  return __uint_as_float(t[0]) + __uint_as_float(t[1]);
}

template <int CTRL> __device__ __forceinline__ unsigned dppu(unsigned x) {
  return (unsigned)__builtin_amdgcn_mov_dpp((int)x, CTRL, 0xf, 0xf, true);
}
__device__ __forceinline__ unsigned wave_umax(unsigned m) {
  m = max(m, dppu<0xB1>(m));
  m = max(m, dppu<0x4E>(m));
  m = max(m, dppu<0x141>(m));
  m = max(m, dppu<0x128>(m));
  auto s = __builtin_amdgcn_permlane16_swap(m, m, false, false);
  m = max(s[0], s[1]);
  auto t = __builtin_amdgcn_permlane32_swap(m, m, false, false);
  m = max(t[0], t[1]);
  return __builtin_amdgcn_readfirstlane(m);
}

__device__ __forceinline__ unsigned wave_usum(unsigned m) {
  m += dppu<0xB1>(m);
  m += dppu<0x4E>(m);
  m += dppu<0x141>(m);
  m += dppu<0x128>(m);
  auto s = __builtin_amdgcn_permlane16_swap(m, m, false, false);
  m = s[0] + s[1];
  auto t = __builtin_amdgcn_permlane32_swap(m, m, false, false);
  m = t[0] + t[1];
  return __builtin_amdgcn_readfirstlane(m);
}

struct Frag { u32x4 t[6]; };
struct VT { u32x2 e[12]; };
struct OB { u32x4 a[4]; u32x2 b[4]; };

__device__ __forceinline__ void read_frag(Frag& f, const char* lds, int aE, int aO) {
#pragma unroll
  for (int t = 0; t < 6; ++t) f.t[t] = *(const u32x4*)(lds + ((t & 1) ? aO : aE) + (t >> 1) * LT);
}
__device__ __forceinline__ void read_vt(VT& o, const char* lds, int aA, int aB) {
  u32x4 xa[4];
  u32x2 xb[4];
#pragma unroll
  for (int i = 0; i < 4; ++i) {
    xa[i] = *(const u32x4*)(lds + aA + i * 128);
    xb[i] = *(const u32x2*)(lds + aB + i * 128);
  }
#pragma unroll
  for (int d = 0; d < 6; ++d) {
    unsigned x[4];
#pragma unroll
    for (int i = 0; i < 4; ++i) x[i] = d < 4 ? xa[i][d] : xb[i][d - 4];
    o.e[2 * d][0] = __builtin_amdgcn_perm(x[1], x[0], 0x05040100u);
    o.e[2 * d][1] = __builtin_amdgcn_perm(x[3], x[2], 0x05040100u);
    o.e[2 * d + 1][0] = __builtin_amdgcn_perm(x[1], x[0], 0x07060302u);
    o.e[2 * d + 1][1] = __builtin_amdgcn_perm(x[3], x[2], 0x07060302u);
  }
}
__device__ __forceinline__ f32x4 qk(const Frag& kf, const Frag& qf, f32x4 acc) {
#pragma unroll
  for (int t = 0; t < 6; ++t)
    acc = __builtin_amdgcn_mfma_f32_16x16x32_bf16(__builtin_bit_cast(bf16x8, kf.t[t]), __builtin_bit_cast(bf16x8, qf.t[t]), acc, 0, 0, 0);
  return acc;
}
__device__ __forceinline__ u32x4 softmax8(f32x4 s0, f32x4 s1) {
  constexpr float C2 = 0.07216878364870322f * 1.4426950408889634f;
  float m = fmaxf(fmaxf(fmaxf(s0[0], s0[1]), fmaxf(s0[2], s0[3])), fmaxf(fmaxf(s1[0], s1[1]), fmaxf(s1[2], s1[3])));
  m = xg_max(m);
  float e[8];
#pragma unroll
  for (int i = 0; i < 4; ++i) {
    e[i] = __builtin_amdgcn_exp2f((s0[i] - m) * C2);
    e[4 + i] = __builtin_amdgcn_exp2f((s1[i] - m) * C2);
  }
  float sum = ((e[0] + e[1]) + (e[2] + e[3])) + ((e[4] + e[5]) + (e[6] + e[7]));
  sum = xg_sum(sum);
  const float inv = __builtin_amdgcn_rcpf(sum);
  u32x4 p;
#pragma unroll
  for (int i = 0; i < 4; ++i) p[i] = pack_bf16(e[2 * i] * inv, e[2 * i + 1] * inv);
  return p;
}
__device__ __forceinline__ void pv(OB& o, u32x4 p, const VT& ta, const VT& tb) {
  f32x4 acc[12];
#pragma unroll
  for (int e = 0; e < 12; ++e) {
    const u32x4 bb = {ta.e[e][0], ta.e[e][1], tb.e[e][0], tb.e[e][1]};
    acc[e] = __builtin_amdgcn_mfma_f32_16x16x32_bf16(__builtin_bit_cast(bf16x8, p), __builtin_bit_cast(bf16x8, bb), f32x4{0.f, 0.f, 0.f, 0.f}, 0, 0, 0);
  }
#pragma unroll
  for (int i = 0; i < 4; ++i) {
    o.a[i] = u32x4{pack_bf16(acc[0][i], acc[1][i]), pack_bf16(acc[2][i], acc[3][i]), pack_bf16(acc[4][i], acc[5][i]), pack_bf16(acc[6][i], acc[7][i])};
    o.b[i] = u32x2{pack_bf16(acc[8][i], acc[9][i]), pack_bf16(acc[10][i], acc[11][i])};
  }
}

__global__ void __launch_bounds__(256, 2)
swa_kernel(const void* q, unsigned qn, unsigned qf, const void* k, unsigned kn, unsigned kf,
           const void* v, unsigned vn, unsigned vf, void* o, void* hint) {
  __shared__ __attribute__((aligned(16))) char lds[4 * RING * US + 2048];
  SB();
  const int lane = threadIdx.x & 63;
  unsigned cpf0, cpf1;
  {
    unsigned long long pc;
    unsigned len;
    asm volatile("s_getpc_b64 %0\n.Lswa_code_begin:\n\ts_mov_b32 %1, .Lswa_code_end-.Lswa_code_begin-4" : "=s"(pc), "=s"(len));
    const char* cp = (const char*)pc;
    cpf0 = *(const __attribute__((address_space(1))) unsigned*)(cp + min((unsigned)lane * 128u, len));
    cpf1 = *(const __attribute__((address_space(1))) unsigned*)(cp + min((unsigned)lane * 128u + 8192u, len));
  }
  const int r = __builtin_amdgcn_readfirstlane(threadIdx.x >> 6);
  const int b = blockIdx.x;
  const int n = (b & 7) * 32 + (b >> 3);
  const int h = n >> 3, u = n & 7;
  const bool rev = (u & 1);
  const bool first = (u == 0);
  const int hb = h * (S * ROWB);
  const int rb = hb + r * ROWB;
  const int j0 = 64 * u;

  const int ustep = rev ? -8 * JB : 8 * JB;
  const int sk = rb + (rev ? j0 + 56 : j0 - 8) * JB;
  const int sq = rb + (rev ? j0 + 56 : j0) * JB;
  const int sk0 = first ? hb : sk;
  const int li = lane >> 3, lp = lane & 7;
  const int rowoff = (rev ? 7 - li : li) * JB;
  const int vKQ0 = rowoff + ((lp ^ (li >> 1)) << 4);
  const int vKQ1 = vKQ0 ^ 64;
  const int vV = rowoff + (lp << 4);
  const int vK0 = first ? ((lp ^ (li >> 1)) << 4) : vKQ0;
  const int vV0 = first ? (lp << 4) : vV;
  const unsigned lds0 = (unsigned)(size_t)(__attribute__((address_space(3))) char*)lds;
  const unsigned lbase = lds0 + r * (RING * US);

  const int g = lane >> 4, c = lane & 15;
  int aE, aO, aEd, aOd, aVa, aVb, aVad, aVbd, va, vb;
  f32x4 biasA, biasA0, biasB;
  const int istep = rev ? -JB : JB;
  const int so = rb + (rev ? j0 + 51 : j0) * JB;
#define SETUP()                                                                                  \
  {                                                                                              \
    const int wb = r * (RING * US);                                                              \
    aEd = wb + (c & 7) * 128 + ((g ^ ((c & 7) >> 1)) << 4);                                      \
    aOd = aEd ^ 64;                                                                              \
    aE = wb + (c >> 3) * US + (c & 7) * 128 + ((g ^ (c >> 1)) << 4);                             \
    aO = aE ^ 64;                                                                                \
    aVad = wb + (c >> 3) * LT + (g & 1) * 512 + (c & 7) * 16;                                    \
    aVbd = wb + 2 * LT + (g & 1) * 512 + c * 8;                                                  \
    aVa = aVad + (g >> 1) * US;                                                                  \
    aVb = aVbd + (g >> 1) * US;                                                                  \
    const int vrow = (rev ? 12 - 4 * g : 4 * g) * JB;                                            \
    va = vrow + 16 * c;                                                                          \
    vb = vrow + 256 + 8 * c;                                                                     \
    const int d0 = 4 * g - c - (rev ? 0 : 1);                                                    \
    const float NEG = -__builtin_inff();                                                         \
    const float pos_mask = (first && g < 2) ? -1e9f * 13.856406460551018f : 0.f;                 \
    _Pragma("unroll") for (int i = 0; i < 4; ++i) {                                              \
      const bool inA = (unsigned)(d0 + i) <= 7u, inB = (unsigned)(d0 + 16 + i) <= 7u;            \
      biasA[i] = inA ? 0.f : NEG;                                                                \
      biasA0[i] = inA ? pos_mask : NEG;                                                          \
      biasB[i] = inB ? 0.f : NEG;                                                                \
    }                                                                                            \
  }
#define KEEP(x) asm volatile("" : "+v"(x))
#define PIN(OO) _Pragma("unroll") for (int i_ = 0; i_ < 4; ++i_) asm volatile("" : "+v"(OO.a[i_]), "+v"(OO.b[i_]))
#define STORE_ROW(OO, blk, i)                                                                    \
  {                                                                                              \
    __builtin_amdgcn_raw_buffer_store_b128(OO.a[i], ro, va, so + (16 * (blk) + (i)) * istep, 16); \
    __builtin_amdgcn_raw_buffer_store_b64(OO.b[i], ro, vb, so + (16 * (blk) + (i)) * istep, 16);  \
    SB();                                                                                        \
  }

  unsigned tgate;
  constexpr int NREP = 8, RSTRIDE = 256;
  const int vpub = (((b >> 3) == r * 9 + 2) && lane < NREP) ? lane * RSTRIDE : OOB;
  const int spub = ((b & 7) * 4 + r) * 4;
  const unsigned lhint = lds0 + 4 * RING * US + r * 512;
  const int ahint = 4 * RING * US + r * 512 + lane * 4;
#define PUBLISH(SCHED)         \
  {                                                                                              \
    const unsigned lag = CLOCK() - torg - (SCHED);                                               \
    asm volatile("buffer_store_dword %0, %1, %2, %3 offen sc1"                                   \
                 :: "v"(torg + 1000u + ((lag * 6u) >> 2)), "v"(vpub), "s"(rw), "s"(spub) : "memory"); \
    SB();                                                                                        \
  }
#define POLL()                                                                                   \
  asm volatile("s_mov_b32 m0, %0\n\ts_nop 0\n\tbuffer_load_dword %1, %2, %3 offen sc1 lds"       \
               :: "s"(lhint), "v"((lane & 31) * 4), "s"(rw), "s"((b & (NREP - 1)) * RSTRIDE) : "memory")
#define GATE()                                                                                   \
  {                                                                                              \
    const unsigned lim = CLOCK() - torg + 200u;                                                \
    unsigned d = *(const unsigned*)(lds + ahint) - torg;                                       \
    d = d < 4000u ? d : lim;                                                                     \
    const unsigned mean = wave_usum(d) >> 6;                                                     \
    d += dppu<0xB1>(d);                                                                          \
    d += dppu<0x4E>(d);                                                                          \
    const unsigned slowest = wave_umax(d) >> 2;                         \
    tgate = torg + min(lim, mean + ((slowest - mean) >> 1));                                   \
    WAIT_UNTIL(tgate);                                                                           \
  }

  Frag Q0, Q1, Q2, Q3, Kf;
  VT T0, T1, T2, T3, T4;
  OB O0, O1, O2, O3;
  f32x4 SA;
  u32x4 P0, P1, P2, P3;
  asm volatile("" :: "s"(sq), "s"(sq + ustep), "s"(sq + 2 * ustep), "s"(sq + 3 * ustep), "s"(lbase), "v"(vKQ0), "v"(vKQ1));
  SB();
#define SRD(p, n, f) u32x4{(unsigned)(size_t)(p), (unsigned)((size_t)(p) >> 32), (n), (f)}
  const u32x4 rk = SRD(k, kn, kf), rq = SRD(q, qn, qf), rv = SRD(v, vn, vf);
#define LATE_SETUP()                                                                             \
  SB();                                                                                          \
  asm volatile("" : "+s"(o), "+s"(hint) : "s"(rk), "s"(rv));                                           \
  const __amdgpu_buffer_rsrc_t ro = __builtin_amdgcn_make_buffer_rsrc(o, 0, TOTAL_BYTES, SRD_FLAGS); \
  const u32x4 rw = SRD(hint, (unsigned)(NREP * RSTRIDE), SRD_FLAGS);                             \
  SETUP();                                                                                       \
  asm volatile("" : "+v"(aE), "+v"(aO), "+v"(aVa), "+v"(aVb), "+v"(biasA), "+v"(biasA0), "+v"(biasB)); \
  SB()

  asm volatile("" :: "s"(rq)); SB();
  const unsigned torg = CLOCK(); SB();
  GLDS_NT(vKQ0, rq, sq + 0 * ustep, lbase + 0);
  GLDS_NT(vKQ1, rq, sq + 1 * ustep, lbase + 3328);
  GLDS_NT(vKQ0, rq, sq + 2 * ustep, lbase + 6656);
  GLDS_NT(vKQ1, rq, sq + 3 * ustep, lbase + 9984);
  LATE_SETUP();
  WAITV(9);
  WAIT_UNTIL(torg + 128u);
  GLDS_NT(vKQ0, rq, sq + 4 * ustep, lbase + 13312);
  SB();
  WAITV(9);
  read_frag(Q0, lds, aE + 0, aO + 0);
  LGKM0();
  SB();
  WAIT_UNTIL(torg + 170u);
  GLDS_NT(vKQ1, rq, sq + 5 * ustep, lbase + 16640);
  SB();
  WAITV(9);
  WAIT_UNTIL(torg + 212u);
  GLDS_NT(vKQ0, rq, sq + 6 * ustep, lbase + 0);
  SB();
  WAITV(9);
  read_frag(Q1, lds, aE + 6656, aO + 6656);
  LGKM0();
  SB();
  WAIT_UNTIL(torg + 254u);
  GLDS_NT(vKQ1, rq, sq + 7 * ustep, lbase + 3328);
  SB();
  WAITV(9);
  WAIT_UNTIL(torg + 296u);
  GLDS(vK0, rk, sk0, lbase + 6656);
  SB();
  WAITV(9);
  read_frag(Q2, lds, aE + 13312, aO + 13312);
  LGKM0();
  SB();
  WAIT_UNTIL(torg + 317u);
  GLDS_NT(vKQ1, rk, sk + 1 * ustep, lbase + 9984);
  SB();
  WAITV(9);
  WAIT_UNTIL(torg + 359u);
  GLDS_NT(vKQ0, rk, sk + 2 * ustep, lbase + 13312);
  SB();
  WAITV(9);
  read_frag(Q3, lds, aE + 0, aO + 0);
  LGKM0();
  SB();
  WAIT_UNTIL(torg + 401u);
  GLDS_NT(vKQ1, rk, sk + 3 * ustep, lbase + 16640);
  SB();
  WAITV(9);
  WAIT_UNTIL(torg + 443u);
  GLDS_NT(vKQ0, rk, sk + 4 * ustep, lbase + 0);
  SB();
  WAITV(9);
  read_frag(Kf, lds, aE + 6656, aO + 6656);
  SA = qk(Kf, Q0, biasA0);
  KEEP(SA);
  LGKM0();
  SB();
  WAIT_UNTIL(torg + 485u);
  GLDS_NT(vKQ1, rk, sk + 5 * ustep, lbase + 3328);
  SB();
  WAITV(9);
  WAIT_UNTIL(torg + 527u);
  GLDS_NT(vKQ0, rk, sk + 6 * ustep, lbase + 6656);
  SB();
  WAITV(9);
  read_frag(Kf, lds, aE + 13312, aO + 13312);
  P0 = softmax8(SA, qk(Kf, Q0, biasB));
  KEEP(P0);
  SA = qk(Kf, Q1, biasA);
  KEEP(SA);
  LGKM0();
  SB();
  WAIT_UNTIL(torg + 569u);
  GLDS_NT(vKQ1, rk, sk + 7 * ustep, lbase + 9984);
  SB();
  WAITV(9);
  WAIT_UNTIL(torg + 611u);
  GLDS(vKQ0, rk, sk + 8 * ustep, lbase + 13312);
  SB();
  WAITV(9);
  read_frag(Kf, lds, aE + 0, aO + 0);
  P1 = softmax8(SA, qk(Kf, Q1, biasB));
  KEEP(P1);
  SA = qk(Kf, Q2, biasA);
  KEEP(SA);
  LGKM0();
  SB();
  WAIT_UNTIL(torg + 632u);
  GLDS(vV0, rv, sk0, lbase + 0);
  SB();
  WAITV(9);
  WAIT_UNTIL(torg + 653u);
  GLDS_NT(vV, rv, sk + 1 * ustep, lbase + 3328);
  SB();
  WAITV(9);
  PUBLISH(653u);
  read_frag(Kf, lds, aE + 6656, aO + 6656);
  P2 = softmax8(SA, qk(Kf, Q2, biasB));
  KEEP(P2);
  SA = qk(Kf, Q3, biasA);
  KEEP(SA);
  LGKM0();
  SB();
  WAIT_UNTIL(torg + 695u);
  GLDS_NT(vV, rv, sk + 2 * ustep, lbase + 6656);
  SB();
  WAITV(9);
  read_frag(Kf, lds, aEd + 13312, aOd + 13312);
  P3 = softmax8(SA, qk(Kf, Q3, biasB));
  KEEP(P3);
  LGKM0();
  SB();
  WAIT_UNTIL(torg + 737u);
  GLDS_NT(vV, rv, sk + 3 * ustep, lbase + 9984);
  SB();
  WAITV(9);
  WAIT_UNTIL(torg + 779u);
  GLDS_NT(vV, rv, sk + 4 * ustep, lbase + 13312);
  SB();
  WAITV(9);
  read_vt(T0, lds, aVa + 0, aVb + 0);
  LGKM0();
  SB();
  WAIT_UNTIL(torg + 821u);
  GLDS_NT(vV, rv, sk + 5 * ustep, lbase + 16640);
  SB();
  WAITV(9);
  WAIT_UNTIL(torg + 863u);
  GLDS_NT(vV, rv, sk + 6 * ustep, lbase + 0);
  SB();
  WAITV(9);
  read_vt(T1, lds, aVa + 6656, aVb + 6656);
  pv(O0, P0, T0, T1);
  PIN(O0);
  LGKM0();
  SB();
  WAIT_UNTIL(torg + 905u);
  POLL();
  GLDS_NT(vV, rv, sk + 7 * ustep, lbase + 3328);
  SB();
  WAITV(10);
  WAIT_UNTIL(torg + 947u);
  GLDS(vV, rv, sk + 8 * ustep, lbase + 6656);
  SB();
  WAITV(10);
  read_vt(T2, lds, aVa + 13312, aVb + 13312);
  pv(O1, P1, T1, T2);
  PIN(O1);
  LGKM0();
  SB();
  WAITV(7);
  SB();
  WAITV(3);
  read_vt(T3, lds, aVa + 0, aVb + 0);
  pv(O2, P2, T2, T3);
  PIN(O2);
  LGKM0();
  SB();
  WAITV(0);
  asm volatile("" :: "v"(cpf0), "v"(cpf1));
  GATE();
  STORE_ROW(O0, 0, 0);
  WAIT_UNTIL(tgate + 19u);
  STORE_ROW(O0, 0, 1);
  WAIT_UNTIL(tgate + 38u);
  STORE_ROW(O0, 0, 2);
  WAIT_UNTIL(tgate + 57u);
  STORE_ROW(O0, 0, 3);
  STORE_ROW(O1, 1, 0);
  STORE_ROW(O1, 1, 1);
  read_vt(T4, lds, aVad + 6656, aVbd + 6656);
  pv(O3, P3, T3, T4);
  PIN(O3);
  LGKM0();
  SB();
  WAIT_UNTIL(tgate + 114u);
  STORE_ROW(O1, 1, 2);
  WAIT_UNTIL(tgate + 133u);
  STORE_ROW(O1, 1, 3);
  WAIT_UNTIL(tgate + 152u);
  STORE_ROW(O2, 2, 0);
  WAIT_UNTIL(tgate + 171u);
  STORE_ROW(O2, 2, 1);
  WAIT_UNTIL(tgate + 190u);
  STORE_ROW(O2, 2, 2);
  WAIT_UNTIL(tgate + 209u);
  STORE_ROW(O2, 2, 3);
  WAIT_UNTIL(tgate + 228u);
  STORE_ROW(O3, 3, 0);
  WAIT_UNTIL(tgate + 247u);
  STORE_ROW(O3, 3, 1);
  WAIT_UNTIL(tgate + 266u);
  STORE_ROW(O3, 3, 2);
  WAIT_UNTIL(tgate + 285u);
  STORE_ROW(O3, 3, 3);
  asm volatile(".Lswa_code_end:" ::: "memory");
}

extern "C" void kernel_launch(void* const* d_in, const int* in_sizes, int n_in,
                              void* d_out, int out_size, void* d_ws, size_t ws_size,
                              hipStream_t stream) {
  swa_kernel<<<256, 256, 0, stream>>>(d_in[0], TOTAL_BYTES, SRD_FLAGS, d_in[1], TOTAL_BYTES, SRD_FLAGS,
                                      d_in[2], TOTAL_BYTES, SRD_FLAGS, d_out, d_ws);
}
```
